```python
import torch

def _softmax(x, axis=-1):
    return torch.softmax(x, dim=axis)
def _gelu(x):
    return torch.nn.functional.gelu(x, approximate="tanh")
def _silu(x):
    return torch.nn.functional.silu(x)
def _sigmoid(x):
    return torch.sigmoid(x)
def _layernorm(x, axis=-1, eps=1e-5):
    m = x.mean(dim=axis, keepdim=True)
    v = x.var(dim=axis, keepdim=True, unbiased=False)
    return (x - m) / torch.sqrt(v + eps)
def _scatter_add(tgt, idx, src, axis=0):
    return tgt.clone().index_add_(axis, idx, src)
def _segment_sum(src, seg, n):
    out = torch.zeros((n,) + src.shape[1:], dtype=src.dtype)
    return out.index_add_(0, seg, src)
def _pad(x, pw):
    flat = tuple(v for pair in reversed(pw) for v in pair)
    return torch.nn.functional.pad(x, flat)

kernel_name = 'parallel_sum__sliding_block__moe_arch_mod__prompt'

def setup_inputs():

    _w = (lambda a: (lambda t: t.to(torch.bfloat16) if t.is_floating_point() else t)(torch.as_tensor(a)))
    def _A_setup():
        B, S, D = 1, 2048, 448
        return {
            "x": _w((lambda *s: torch.randn(*s, dtype=torch.bfloat16) * 0.05)(B, S, D)),
            "wqkv": _w((lambda *s: torch.randn(*s, dtype=torch.bfloat16) * 0.05)(D, 3 * D)),
            "wo": _w((lambda *s: torch.randn(*s, dtype=torch.bfloat16) * 0.05)(D, D)),
        }
    _A_in = _A_setup()

    def _B_setup():
        B, S, D, F, C = 1, 2048, 448, 1792, 2
        return {
            "x": _w((lambda *s: torch.randn(*s, dtype=torch.bfloat16) * 0.05)(B, S, D)),
            "w_r": _w((lambda *s: torch.randn(*s, dtype=torch.bfloat16) * 0.05)(D)),
            "w_gate": _w((lambda *s: torch.randn(*s, dtype=torch.bfloat16) * 0.05)(D, F)),
            "w_up": _w((lambda *s: torch.randn(*s, dtype=torch.bfloat16) * 0.05)(D, F)),
            "w_down": _w((lambda *s: torch.randn(*s, dtype=torch.bfloat16) * 0.05)(F, D)),
        }
    _B_in = _B_setup()

    return {
        "A_x": _A_in["x"],
        "A_wqkv": _A_in["wqkv"],
        "A_wo": _A_in["wo"],
        "B_x": _B_in["x"],
        "B_w_r": _B_in["w_r"],
        "B_w_gate": _B_in["w_gate"],
        "B_w_up": _B_in["w_up"],
        "B_w_down": _B_in["w_down"],
    }

def reference(A_x, A_wqkv, A_wo, B_x, B_w_r, B_w_gate, B_w_up, B_w_down):
    def _A_ref(x, wqkv, wo):
        B, S, D = x.shape
        H = 16; Dh = D // H; W = 256
        qkv = (lambda x, *d: x.permute(*d))((x @ wqkv).reshape(B, S, 3, H, Dh), 2, 0, 3, 1, 4)
        q, k, v = qkv[0], qkv[1], qkv[2]
        scores = torch.einsum("bhqd,bhkd->bhqk", q, k) * (Dh ** -0.5)
        idx = torch.arange(S)
        mask = (idx[:, None] - idx[None, :] >= 0) & (idx[:, None] - idx[None, :] < W)
        scores = torch.where(mask[None, None], scores, -1e9)
        p = _softmax(scores, axis=-1)
        o = torch.einsum("bhqk,bhkd->bhqd", p, v)
        return (lambda x, *d: x.permute(*d))(o, 0, 2, 1, 3).reshape(B, S, D) @ wo

    def _B_ref(x, w_r, w_gate, w_up, w_down):
        B, S, D = x.shape
        C = 2
        r = x @ w_r
        idx = torch.argsort(-r, axis=-1)[:, :C]
        idx_e = (lambda x, n, axis: torch.repeat_interleave(x, n, dim=axis))(idx.reshape(B, C, 1), D, axis=2)
        x_sel = (lambda x, i, axis: torch.take_along_dim(x, i, dim=axis))(x, idx_e, axis=1)
        g = _silu(x_sel @ w_gate)
        u = x_sel @ w_up
        y = (g * u) @ w_down
        r_sel = _sigmoid((lambda x, i, axis: torch.take_along_dim(x, i, dim=axis))(r, idx, axis=1))
        y = y * r_sel.reshape(B, C, 1)
        b_off = (torch.arange(B) * S).reshape(B, 1)
        flat_idx = (idx + b_off).reshape(B * C)
        zero = (x * 0.0).reshape(B * S, D)
        upd = _scatter_add(zero, flat_idx, y.reshape(B * C, D), axis=0)
        return x + upd.reshape(B, S, D)

    x = A_x
    xn = _layernorm(x, axis=-1)
    return x + _A_ref(xn, A_wqkv, A_wo) + _B_ref(xn, B_w_r, B_w_gate, B_w_up, B_w_down)
```

```cpp
#include <hip/hip_runtime.h>
#include <hip/hip_bf16.h>
#include <stdint.h>

typedef unsigned short u16;
typedef short s16x4 __attribute__((ext_vector_type(4)));
typedef short bf16x8 __attribute__((ext_vector_type(8)));
typedef float f32x4 __attribute__((ext_vector_type(4)));
typedef unsigned u32x4 __attribute__((ext_vector_type(4)));
typedef unsigned u32x2 __attribute__((ext_vector_type(2)));
typedef __attribute__((address_space(3))) s16x4 lds_s16x4;

#define DI __device__ __forceinline__
#define SEQ 2048
#define DM 448
#define NQKV 1344
#define NH 16
#define DH 28
#define FF 1792
#define WIN 256

DI float bf2f(unsigned v) { return __uint_as_float(v << 16); }
DI float bflo(unsigned v) { return __uint_as_float(v << 16); }
DI float bfhi(unsigned v) { return __uint_as_float(v & 0xffff0000u); }
DI u16 f2bf(float f) { __bf16 b = (__bf16)f; return __builtin_bit_cast(u16, b); }
DI float rbf(float f) { return bf2f(f2bf(f)); }
DI unsigned pk2(float a, float b) { return (unsigned)f2bf(a) | ((unsigned)f2bf(b) << 16); }

#define MFMA16(a, b, c) __builtin_amdgcn_mfma_f32_16x16x32_bf16((a), (b), (c), 0, 0, 0)

DI bf16x8 tr_frag(const u16* base, int row_stride_elems, int lane, int krow0, int col0) {
  const int g = lane >> 4, q = (lane & 15) >> 2, p = lane & 3;
  const u16* a0 = base + (krow0 + 8 * g + q) * row_stride_elems + col0 + 4 * p;
  s16x4 lo = __builtin_amdgcn_ds_read_tr16_b64_v4i16((lds_s16x4*)(a0));
  s16x4 hi = __builtin_amdgcn_ds_read_tr16_b64_v4i16((lds_s16x4*)(a0 + 4 * row_stride_elems));
  return __builtin_shufflevector(lo, hi, 0, 1, 2, 3, 4, 5, 6, 7);
}

#define K1_BN 96
__global__ __launch_bounds__(256) void k1_ln_qkv(const u16* __restrict__ x, const u16* __restrict__ wqkv,
                                                 const u16* __restrict__ w_r, u16* __restrict__ xn_out,
                                                 float* __restrict__ r_out, u16* __restrict__ Qp,
                                                 u16* __restrict__ Kp, u16* __restrict__ Vt) {
  __shared__ __attribute__((aligned(16))) u16 wt[DM * K1_BN];
  const int tid = threadIdx.x, lane = tid & 63, wid = tid >> 6;
  const int nt = blockIdx.x % 14, rt = blockIdx.x / 14;
  const int n_base = nt * K1_BN;
  const int g = lane >> 4, l15 = lane & 15;

  {
    u32x4 tmp[21];
#pragma unroll
    for (int i = 0; i < 21; ++i) {
      const int id = i * 256 + tid;
      const int row = id / 12, ch = id % 12;
      tmp[i] = *(const u32x4*)(wqkv + (size_t)row * NQKV + n_base + ch * 8);
    }
#pragma unroll
    for (int i = 0; i < 21; ++i) {
      const int id = i * 256 + tid;
      *(u32x4*)(wt + id * 8) = tmp[i];
    }
  }

  bf16x8 xf[2][14];
#pragma unroll
  for (int t = 0; t < 2; ++t) {
    const int row = rt * 128 + wid * 32 + t * 16 + l15;
    u32x4 raw[14];
#pragma unroll
    for (int s = 0; s < 14; ++s) raw[s] = *(const u32x4*)(x + (size_t)row * DM + s * 32 + g * 8);
    float sum = 0.f;
#pragma unroll
    for (int s = 0; s < 14; ++s)
#pragma unroll
      for (int j = 0; j < 4; ++j) sum += bflo(raw[s][j]) + bfhi(raw[s][j]);
    sum += __shfl_xor(sum, 16);
    sum += __shfl_xor(sum, 32);
    const float mean = sum * (1.0f / DM);
    float vs = 0.f;
#pragma unroll
    for (int s = 0; s < 14; ++s)
#pragma unroll
      for (int j = 0; j < 4; ++j) {
        const float a = bflo(raw[s][j]) - mean, b = bfhi(raw[s][j]) - mean;
        vs += a * a + b * b;
      }
    vs += __shfl_xor(vs, 16);
    vs += __shfl_xor(vs, 32);
    const float var = vs * (1.0f / DM);
    const float mb = rbf(mean);
    const float den = rbf(sqrtf(rbf(rbf(var) + 1e-5f)));
    float rdot = 0.f;
#pragma unroll
    for (int s = 0; s < 14; ++s) {
      u32x4 o;
#pragma unroll
      for (int j = 0; j < 4; ++j) {
        const float a = rbf(bflo(raw[s][j]) - mb) / den;
        const float b = rbf(bfhi(raw[s][j]) - mb) / den;
        o[j] = pk2(a, b);
      }
      xf[t][s] = __builtin_bit_cast(bf16x8, o);
      if (nt == s) *(u32x4*)(xn_out + (size_t)row * DM + s * 32 + g * 8) = o;
    }
    if (nt == 0) {
#pragma unroll
      for (int s = 0; s < 14; ++s) {
        const u32x4 wr = *(const u32x4*)(w_r + s * 32 + g * 8);
        const u32x4 o = __builtin_bit_cast(u32x4, xf[t][s]);
#pragma unroll
        for (int j = 0; j < 4; ++j) rdot += bflo(o[j]) * bflo(wr[j]) + bfhi(o[j]) * bfhi(wr[j]);
      }
      rdot += __shfl_xor(rdot, 16);
      rdot += __shfl_xor(rdot, 32);
      if (g == 0) r_out[row] = rbf(rdot);
    }
  }
  __syncthreads();

  f32x4 acc[2][6];
#pragma unroll
  for (int t = 0; t < 2; ++t)
#pragma unroll
    for (int c = 0; c < 6; ++c) acc[t][c] = (f32x4){0.f, 0.f, 0.f, 0.f};

#pragma unroll
  for (int c = 0; c < 6; ++c) {
    const bool isv = (n_base + 16 * c) >= 2 * DM;
    if (isv) {
#pragma unroll
      for (int s = 0; s < 14; ++s) {
        const bf16x8 wf = tr_frag(wt, K1_BN, lane, 32 * s, 16 * c);
        acc[0][c] = MFMA16(xf[0][s], wf, acc[0][c]);
        acc[1][c] = MFMA16(xf[1][s], wf, acc[1][c]);
      }
    } else {
#pragma unroll
      for (int s = 0; s < 14; ++s) {
        const bf16x8 wf = tr_frag(wt, K1_BN, lane, 32 * s, 16 * c);
        acc[0][c] = MFMA16(wf, xf[0][s], acc[0][c]);
        acc[1][c] = MFMA16(wf, xf[1][s], acc[1][c]);
      }
    }
  }

#pragma unroll
  for (int c = 0; c < 6; ++c) {
    const int n0 = n_base + 16 * c;
#pragma unroll
    for (int t = 0; t < 2; ++t) {
      const int rbase = rt * 128 + wid * 32 + t * 16;
      u32x2 pk;
      pk[0] = pk2(acc[t][c][0], acc[t][c][1]);
      pk[1] = pk2(acc[t][c][2], acc[t][c][3]);
      if (n0 >= 2 * DM) {
        const int nn = n0 + l15 - 2 * DM;
        const int h = nn / DH, d = nn % DH;
        *(u32x2*)(Vt + ((size_t)(h * 32 + d) * SEQ + rbase + 4 * g)) = pk;
      } else {
        const int n = n0 + 4 * g;
        const int which = n >= DM;
        const int nn = n - which * DM;
        const int h = nn / DH, d0 = nn % DH;
        u16* dst = (which ? Kp : Qp) + ((size_t)(h * SEQ + rbase + l15) * 32 + d0);
        if (d0 == 24) {
          u32x4 p4 = {pk[0], pk[1], 0u, 0u};
          *(u32x4*)dst = p4;
        } else {
          *(u32x2*)dst = pk;
        }
      }
    }
  }
}

#define KV_KEYS 320
#define VSTR 328
#define NFS 56

__global__ __launch_bounds__(256) void k2_attn_ffn(const u16* __restrict__ Qp, const u16* __restrict__ Kp,
                                                   const u16* __restrict__ Vt, u16* __restrict__ o_out,
                                                   const u16* __restrict__ xn, const float* __restrict__ r,
                                                   const u16* __restrict__ wg, const u16* __restrict__ wu,
                                                   const u16* __restrict__ wd, float* __restrict__ ypart,
                                                   int* __restrict__ sel_out) {
  __shared__ __attribute__((aligned(16))) u16 smem[KV_KEYS * 32 + 32 * VSTR];
  const int tid = threadIdx.x, lane = tid & 63, wid = tid >> 6;
  const int g = lane >> 4, l15 = lane & 15;

  if (blockIdx.x < 512) {
    const int h = blockIdx.x & 15, qb = blockIdx.x >> 4;
    const int q0 = qb * 64;
    const int kbase = q0 - 256;
    u16* Ks = smem;
    u16* Vs = smem + KV_KEYS * 32;
    {
      u32x4 tk[5], tv[5];
#pragma unroll
      for (int i = 0; i < 5; ++i) {
        const int id = i * 256 + tid;
        const int row = id >> 2, ch = id & 3;
        int key = kbase + row;
        key = key < 0 ? 0 : key;
        tk[i] = *(const u32x4*)(Kp + ((size_t)(h * SEQ + key) * 32 + ch * 8));
        const int d = id / 40, c8 = id % 40;
        int k8 = kbase + c8 * 8;
        k8 = k8 < 0 ? 0 : k8;
        tv[i] = *(const u32x4*)(Vt + ((size_t)(h * 32 + d) * SEQ + k8));
      }
#pragma unroll
      for (int i = 0; i < 5; ++i) {
        const int id = i * 256 + tid;
        const int row = id >> 2, ch = id & 3;
        *(u32x4*)(Ks + row * 32 + ((ch ^ ((row >> 2) & 3)) * 8)) = tk[i];
        const int d = id / 40, c8 = id % 40;
        *(u32x4*)(Vs + d * VSTR + c8 * 8) = tv[i];
      }
    }
    const int qw = q0 + wid * 16;
    const bf16x8 qf = *(const bf16x8*)(Qp + ((size_t)(h * SEQ + qw + l15) * 32 + g * 8));
    __syncthreads();

    const int blk0 = (wid >> 1) * 32;
    const int q = qw + l15;
    f32x4 sc[18];
#pragma unroll
    for (int t = 0; t < 18; ++t) {
      const int row = blk0 + t * 16 + l15;
      const bf16x8 kf = *(const bf16x8*)(Ks + row * 32 + ((g ^ ((row >> 2) & 3)) * 8));
      sc[t] = MFMA16(kf, qf, ((f32x4){0.f, 0.f, 0.f, 0.f}));
    }
    float mx = -3.0e38f;
#pragma unroll
    for (int t = 0; t < 18; ++t)
#pragma unroll
      for (int i = 0; i < 4; ++i) {
        const int key = kbase + blk0 + t * 16 + 4 * g + i;
        const int dlt = q - key;
        const bool ok = (key >= 0) && (dlt >= 0) && (dlt < WIN);
        sc[t][i] = ok ? sc[t][i] : -3.0e38f;
        mx = fmaxf(mx, sc[t][i]);
      }
    mx = fmaxf(mx, __shfl_xor(mx, 16));
    mx = fmaxf(mx, __shfl_xor(mx, 32));
    const float cs = 0.18898223650461362f * 1.4426950408889634f;
    float sum = 0.f;
#pragma unroll
    for (int t = 0; t < 18; ++t)
#pragma unroll
      for (int i = 0; i < 4; ++i) {
        const float p = __builtin_amdgcn_exp2f((sc[t][i] - mx) * cs);
        sc[t][i] = p;
        sum += p;
      }
    sum += __shfl_xor(sum, 16);
    sum += __shfl_xor(sum, 32);
    f32x4 o0 = {0.f, 0.f, 0.f, 0.f}, o1 = {0.f, 0.f, 0.f, 0.f};
#pragma unroll
    for (int b = 0; b < 9; ++b) {
      u32x4 pp;
      pp[0] = pk2(sc[2 * b][0], sc[2 * b][1]);
      pp[1] = pk2(sc[2 * b][2], sc[2 * b][3]);
      pp[2] = pk2(sc[2 * b + 1][0], sc[2 * b + 1][1]);
      pp[3] = pk2(sc[2 * b + 1][2], sc[2 * b + 1][3]);
      const bf16x8 pf = __builtin_bit_cast(bf16x8, pp);
      const u16* v0 = Vs + l15 * VSTR + blk0 + b * 32 + 4 * g;
      const u32x2 a0 = *(const u32x2*)(v0);
      const u32x2 a1 = *(const u32x2*)(v0 + 16);
      const u32x2 b0 = *(const u32x2*)(v0 + 16 * VSTR);
      const u32x2 b1 = *(const u32x2*)(v0 + 16 * VSTR + 16);
      const u32x4 va = {a0[0], a0[1], a1[0], a1[1]};
      const u32x4 vb = {b0[0], b0[1], b1[0], b1[1]};
      o0 = MFMA16(__builtin_bit_cast(bf16x8, va), pf, o0);
      o1 = MFMA16(__builtin_bit_cast(bf16x8, vb), pf, o1);
    }
    const float inv = 1.0f / sum;
    u16* dst = o_out + (size_t)q * DM + h * DH;
    u32x2 s0;
    s0[0] = pk2(o0[0] * inv, o0[1] * inv);
    s0[1] = pk2(o0[2] * inv, o0[3] * inv);
    *(u32x2*)(dst + 4 * g) = s0;
    if (g < 3) {
      u32x2 s1;
      s1[0] = pk2(o1[0] * inv, o1[1] * inv);
      s1[1] = pk2(o1[2] * inv, o1[3] * inv);
      *(u32x2*)(dst + 16 + 4 * g) = s1;
    }
    return;
  }

  const int fs = blockIdx.x - 512;
  const int f0 = fs * 32;
  float* sf = (float*)smem;
  float* red = sf + 2 * DM;
  const int fg = tid & 3, kp = tid >> 2;
  u32x4 wgv[7], wuv[7];
#pragma unroll
  for (int i = 0; i < 7; ++i) {
    const int k = kp * 7 + i;
    wgv[i] = *(const u32x4*)(wg + (size_t)k * FF + f0 + fg * 8);
    wuv[i] = *(const u32x4*)(wu + (size_t)k * FF + f0 + fg * 8);
  }
  const int dch = tid % 56, fp = tid / 56;
  u32x4 wdv[8];
  if (fp < 4) {
#pragma unroll
    for (int i = 0; i < 8; ++i) wdv[i] = *(const u32x4*)(wd + (size_t)(f0 + fp * 8 + i) * DM + dch * 8);
  }
  float v1 = -3.0e38f, v2 = -3.0e38f;
  int i1 = 0x7fffffff, i2 = 0x7fffffff;
  {
    const f32x4 ra = *(const f32x4*)(r + tid * 8);
    const f32x4 rb = *(const f32x4*)(r + tid * 8 + 4);
#pragma unroll
    for (int j = 0; j < 8; ++j) {
      const float v = j < 4 ? ra[j & 3] : rb[j & 3];
      const int ix = tid * 8 + j;
      if (v > v1) { v2 = v1; i2 = i1; v1 = v; i1 = ix; }
      else if (v > v2) { v2 = v; i2 = ix; }
    }
#pragma unroll
    for (int m = 1; m < 64; m <<= 1) {
      const float ov1 = __shfl_xor(v1, m), ov2 = __shfl_xor(v2, m);
      const int oi1 = __shfl_xor(i1, m), oi2 = __shfl_xor(i2, m);
      const bool o_first = (ov1 > v1) || (ov1 == v1 && oi1 < i1);
      float a1 = o_first ? ov1 : v1; int ai1 = o_first ? oi1 : i1;
      float c1 = o_first ? v1 : ov1; int ci1 = o_first ? i1 : oi1;
      float c2 = o_first ? ov2 : v2; int ci2 = o_first ? oi2 : i2;
      const bool cfirst = (c1 > c2) || (c1 == c2 && ci1 < ci2);
      v1 = a1; i1 = ai1;
      v2 = cfirst ? c1 : c2; i2 = cfirst ? ci1 : ci2;
    }
    float* rv = red;
    int* ri = (int*)(red + 8);
    if (lane == 0) { rv[wid * 2] = v1; rv[wid * 2 + 1] = v2; ri[wid * 2] = i1; ri[wid * 2 + 1] = i2; }
    __syncthreads();
    v1 = -3.0e38f; v2 = -3.0e38f; i1 = 0x7fffffff; i2 = 0x7fffffff;
#pragma unroll
    for (int j = 0; j < 8; ++j) {
      const float v = rv[j];
      const int ix = ri[j];
      if (v > v1 || (v == v1 && ix < i1)) { v2 = v1; i2 = i1; v1 = v; i1 = ix; }
      else if (v > v2 || (v == v2 && ix < i2)) { v2 = v; i2 = ix; }
    }
    __syncthreads();
  }
  if (fs == 0 && tid == 0) {
    sel_out[0] = i1; sel_out[1] = i2;
    ((float*)sel_out)[2] = rbf(1.0f / (1.0f + __expf(-v1)));
    ((float*)sel_out)[3] = rbf(1.0f / (1.0f + __expf(-v2)));
  }
  if (tid < 112) {
    const int t = tid / 56, c = tid % 56;
    const int row = t ? i2 : i1;
    const u32x4 v = *(const u32x4*)(xn + (size_t)row * DM + c * 8);
#pragma unroll
    for (int j = 0; j < 4; ++j) {
      sf[t * DM + c * 8 + 2 * j] = bflo(v[j]);
      sf[t * DM + c * 8 + 2 * j + 1] = bfhi(v[j]);
    }
  }
  __syncthreads();
  float ag[2][8], au[2][8];
#pragma unroll
  for (int t = 0; t < 2; ++t)
#pragma unroll
    for (int j = 0; j < 8; ++j) { ag[t][j] = 0.f; au[t][j] = 0.f; }
#pragma unroll
  for (int i = 0; i < 7; ++i) {
    const int k = kp * 7 + i;
    const float x0 = sf[k], x1 = sf[DM + k];
#pragma unroll
    for (int j = 0; j < 4; ++j) {
      ag[0][2 * j] += x0 * bflo(wgv[i][j]); ag[0][2 * j + 1] += x0 * bfhi(wgv[i][j]);
      ag[1][2 * j] += x1 * bflo(wgv[i][j]); ag[1][2 * j + 1] += x1 * bfhi(wgv[i][j]);
      au[0][2 * j] += x0 * bflo(wuv[i][j]); au[0][2 * j + 1] += x0 * bfhi(wuv[i][j]);
      au[1][2 * j] += x1 * bflo(wuv[i][j]); au[1][2 * j + 1] += x1 * bfhi(wuv[i][j]);
    }
  }
#pragma unroll
  for (int m = 4; m < 64; m <<= 1)
#pragma unroll
    for (int t = 0; t < 2; ++t)
#pragma unroll
      for (int j = 0; j < 8; ++j) { ag[t][j] += __shfl_xor(ag[t][j], m); au[t][j] += __shfl_xor(au[t][j], m); }
  if (lane < 4) {
#pragma unroll
    for (int t = 0; t < 2; ++t)
#pragma unroll
      for (int j = 0; j < 8; ++j) {
        red[((wid * 2 + t) * 32 + lane * 8 + j) * 2] = ag[t][j];
        red[((wid * 2 + t) * 32 + lane * 8 + j) * 2 + 1] = au[t][j];
      }
  }
  __syncthreads();
  float* hs = red + 1024;
  if (tid < 64) {
    const int t = tid >> 5, f = tid & 31;
    float gsum = 0.f, usum = 0.f;
#pragma unroll
    for (int w = 0; w < 4; ++w) { gsum += red[((w * 2 + t) * 32 + f) * 2]; usum += red[((w * 2 + t) * 32 + f) * 2 + 1]; }
    const float gb = rbf(gsum), ub = rbf(usum);
    const float sg = rbf(gb / (1.0f + __expf(-gb)));
    hs[t * 32 + f] = rbf(sg * ub);
  }
  __syncthreads();
  float* yred = red + 2048;
  if (fp < 4) {
    float y[2][8];
#pragma unroll
    for (int t = 0; t < 2; ++t)
#pragma unroll
      for (int j = 0; j < 8; ++j) y[t][j] = 0.f;
#pragma unroll
    for (int i = 0; i < 8; ++i) {
      const float h0 = hs[fp * 8 + i], h1 = hs[32 + fp * 8 + i];
#pragma unroll
      for (int j = 0; j < 4; ++j) {
        y[0][2 * j] += h0 * bflo(wdv[i][j]); y[0][2 * j + 1] += h0 * bfhi(wdv[i][j]);
        y[1][2 * j] += h1 * bflo(wdv[i][j]); y[1][2 * j + 1] += h1 * bfhi(wdv[i][j]);
      }
    }
#pragma unroll
    for (int t = 0; t < 2; ++t)
#pragma unroll
      for (int j = 0; j < 8; ++j) yred[(fp * 2 + t) * DM + dch * 8 + j] = y[t][j];
  }
  __syncthreads();
  for (int i = tid; i < 2 * DM; i += 256) {
    ypart[(size_t)fs * 2 * DM + i] = yred[i] + yred[2 * DM + i] + yred[4 * DM + i] + yred[6 * DM + i];
  }
}

#define K3_BN 64
__global__ __launch_bounds__(256) void k3_out(const u16* __restrict__ o_in, const u16* __restrict__ wo,
                                              const u16* __restrict__ x, const u16* __restrict__ xn,
                                              const float* __restrict__ ypart, const int* __restrict__ sel,
                                              u16* __restrict__ out) {
  __shared__ __attribute__((aligned(16))) u16 wt[DM * K3_BN];
  __shared__ float updv[2][K3_BN];
  const int tid = threadIdx.x, lane = tid & 63, wid = tid >> 6;
  const int g = lane >> 4, l15 = lane & 15;
  const int nt = blockIdx.x % 7, rt = blockIdx.x / 7;
  const int n_base = nt * K3_BN;
  {
    u32x4 tmp[14];
#pragma unroll
    for (int i = 0; i < 14; ++i) {
      const int id = i * 256 + tid;
      const int row = id >> 3, ch = id & 7;
      tmp[i] = *(const u32x4*)(wo + (size_t)row * DM + n_base + ch * 8);
    }
#pragma unroll
    for (int i = 0; i < 14; ++i) {
      const int id = i * 256 + tid;
      *(u32x4*)(wt + id * 8) = tmp[i];
    }
  }
  const int row = rt * 64 + wid * 16 + l15;
  bf16x8 of[14];
#pragma unroll
  for (int s = 0; s < 14; ++s) of[s] = *(const bf16x8*)(o_in + (size_t)row * DM + s * 32 + g * 8);
  const int s0 = sel[0], s1 = sel[1];
  const bool has0 = (s0 >> 6) == rt, has1 = (s1 >> 6) == rt;
  if (has0 || has1) {
    if (tid < 128) {
      const int t = tid >> 6, c = tid & 63;
      float a = 0.f;
      for (int f = 0; f < NFS; ++f) a += ypart[(size_t)f * 2 * DM + t * DM + n_base + c];
      const float gate = ((const float*)sel)[2 + t];
      updv[t][c] = rbf(rbf(a) * gate);
    }
  }
  __syncthreads();
  f32x4 acc[4];
#pragma unroll
  for (int c = 0; c < 4; ++c) acc[c] = (f32x4){0.f, 0.f, 0.f, 0.f};
#pragma unroll
  for (int c = 0; c < 4; ++c)
#pragma unroll
    for (int s = 0; s < 14; ++s) {
      const bf16x8 wf = tr_frag(wt, K3_BN, lane, 32 * s, 16 * c);
      acc[c] = MFMA16(wf, of[s], acc[c]);
    }
#pragma unroll
  for (int c = 0; c < 4; ++c) {
    const int n = n_base + 16 * c + 4 * g;
    const u32x2 xv = *(const u32x2*)(x + (size_t)row * DM + n);
    const u32x2 nv = *(const u32x2*)(xn + (size_t)row * DM + n);
    float res[4];
#pragma unroll
    for (int i = 0; i < 4; ++i) {
      const float xx = (i & 1) ? bfhi(xv[i >> 1]) : bflo(xv[i >> 1]);
      float bb = (i & 1) ? bfhi(nv[i >> 1]) : bflo(nv[i >> 1]);
      if (row == s0) bb = rbf(bb + updv[0][16 * c + 4 * g + i]);
      if (row == s1) bb = rbf(bb + updv[1][16 * c + 4 * g + i]);
      res[i] = rbf(xx + rbf(acc[c][i])) + bb;
    }
    u32x2 pk;
    pk[0] = pk2(res[0], res[1]);
    pk[1] = pk2(res[2], res[3]);
    *(u32x2*)(out + (size_t)row * DM + n) = pk;
  }
}

extern "C" void kernel_launch(void* const* d_in, const int* in_sizes, int n_in, void* d_out, int out_size,
                              void* d_ws, size_t ws_size, hipStream_t stream) {
  const u16* x = (const u16*)d_in[0];
  const u16* wqkv = (const u16*)d_in[1];
  const u16* wo = (const u16*)d_in[2];
  const u16* w_r = (const u16*)d_in[4];
  const u16* wg = (const u16*)d_in[5];
  const u16* wu = (const u16*)d_in[6];
  const u16* wd = (const u16*)d_in[7];
  u16* out = (u16*)d_out;

  char* ws = (char*)d_ws;
  u16* xn = (u16*)(ws);
  float* r = (float*)(ws + 2 * 1024 * 1024);
  int* sel = (int*)(ws + 2 * 1024 * 1024 + 65536);
  float* ypart = (float*)(ws + 2 * 1024 * 1024 + 131072);
  u16* Qp = (u16*)(ws + 4 * 1024 * 1024);
  u16* Kp = (u16*)(ws + 6 * 1024 * 1024);
  u16* Vt = (u16*)(ws + 8 * 1024 * 1024);
  u16* ow = (u16*)(ws + 10 * 1024 * 1024);

  k1_ln_qkv<<<16 * 14, 256, 0, stream>>>(x, wqkv, w_r, xn, r, Qp, Kp, Vt);
  k2_attn_ffn<<<512 + NFS, 256, 0, stream>>>(Qp, Kp, Vt, ow, xn, r, wg, wu, wd, ypart, sel);
  k3_out<<<32 * 7, 256, 0, stream>>>(ow, wo, x, xn, ypart, sel, out);
}
```

```cpp
#include <hip/hip_runtime.h>
#include <hip/hip_bf16.h>
#include <stdint.h>

typedef unsigned short u16;
typedef short s16x4 __attribute__((ext_vector_type(4)));
typedef short bf16x8 __attribute__((ext_vector_type(8)));
typedef float f32x4 __attribute__((ext_vector_type(4)));
typedef unsigned u32x4 __attribute__((ext_vector_type(4)));
typedef unsigned u32x2 __attribute__((ext_vector_type(2)));
typedef __attribute__((address_space(3))) s16x4 lds_s16x4;

#define DI __device__ __forceinline__
#define SEQ 2048
#define DM 448
#define NQKV 1344
#define NH 16
#define DH 28
#define FF 1792
#define WIN 256

DI float bf2f(unsigned v) { return __uint_as_float(v << 16); }
DI float bflo(unsigned v) { return __uint_as_float(v << 16); }
DI float bfhi(unsigned v) { return __uint_as_float(v & 0xffff0000u); }
DI u16 f2bf(float f) { __bf16 b = (__bf16)f; return __builtin_bit_cast(u16, b); }
DI float rbf(float f) { return bf2f(f2bf(f)); }
DI unsigned pk2(float a, float b) { return (unsigned)f2bf(a) | ((unsigned)f2bf(b) << 16); }

#define MFMA16(a, b, c) __builtin_amdgcn_mfma_f32_16x16x32_bf16((a), (b), (c), 0, 0, 0)

DI bf16x8 tr_frag(const u16* base, int row_stride_elems, int lane, int krow0, int tile, int sw) {
  const int g = lane >> 4, q = (lane & 15) >> 2, p = lane & 3;
  const u16* a0 = base + (krow0 + 8 * g + q) * row_stride_elems + 16 * (tile ^ sw) + 4 * p;
  s16x4 lo = __builtin_amdgcn_ds_read_tr16_b64_v4i16((lds_s16x4*)(a0));
  s16x4 hi = __builtin_amdgcn_ds_read_tr16_b64_v4i16((lds_s16x4*)(a0 + 4 * row_stride_elems));
  return __builtin_shufflevector(lo, hi, 0, 1, 2, 3, 4, 5, 6, 7);
}

#define K1_BN 96
typedef __bf16 bf2v __attribute__((ext_vector_type(2)));
typedef float f32x2 __attribute__((ext_vector_type(2)));
DI float dot2bf(unsigned a, unsigned b, float c) {
  return __builtin_amdgcn_fdot2_f32_bf16(__builtin_bit_cast(bf2v, a), __builtin_bit_cast(bf2v, b), c, false);
}
__global__ __launch_bounds__(512) void k1_ln_qkv(const u16* __restrict__ x, const u16* __restrict__ wqkv,
                                                 const u16* __restrict__ w_r, u16* __restrict__ xn_out,
                                                 float* __restrict__ r_out, u16* __restrict__ Qp,
                                                 u16* __restrict__ Kp, u16* __restrict__ Vt) {
  __shared__ __attribute__((aligned(16))) u16 wt[DM * K1_BN];
  const int tid = threadIdx.x, lane = tid & 63, wid = tid >> 6;
  const int bi = blockIdx.x >> 3;
  const int nt = bi % 14, rt = (blockIdx.x & 7) * 2 + bi / 14;
  const int n_base = nt * K1_BN;
  const int g = lane >> 4, l15 = lane & 15;

  const int row = rt * 128 + wid * 16 + l15;
  u32x4 raw[14];
#pragma unroll
  for (int s = 0; s < 14; ++s) raw[s] = *(const u32x4*)(x + (size_t)row * DM + s * 32 + g * 8);
  u32x4 tmp[11];
#pragma unroll
  for (int i = 0; i < 11; ++i) {
    int id = i * 512 + tid;
    id = id < 5376 ? id : 5375;
    const int wr = id / 12, ch = id % 12;
    tmp[i] = *(const u32x4*)(wqkv + (size_t)wr * NQKV + n_base + ch * 8);
  }
  float sum = 0.f, sq = 0.f;
#pragma unroll
  for (int s = 0; s < 14; ++s)
#pragma unroll
    for (int j = 0; j < 4; ++j) {
      sum = dot2bf(raw[s][j], 0x3f803f80u, sum);
      sq = dot2bf(raw[s][j], raw[s][j], sq);
    }
  sum += __shfl_xor(sum, 16);
  sq += __shfl_xor(sq, 16);
  sum += __shfl_xor(sum, 32);
  sq += __shfl_xor(sq, 32);
  const float mean = sum * (1.0f / DM);
  const float var = fmaxf(sq * (1.0f / DM) - mean * mean, 0.f);
  const float rinv = __frsqrt_rn(var + 1e-5f);
  const float nb = -mean * rinv;
  bf16x8 xf[14];
#pragma unroll
  for (int s = 0; s < 14; ++s) {
    u32x4 o;
#pragma unroll
    for (int j = 0; j < 4; ++j) o[j] = pk2(fmaf(bflo(raw[s][j]), rinv, nb), fmaf(bfhi(raw[s][j]), rinv, nb));
    xf[s] = __builtin_bit_cast(bf16x8, o);
    if (nt == s) *(u32x4*)(xn_out + (size_t)row * DM + s * 32 + g * 8) = o;
  }
  if (nt == 0) {
    float rdot = 0.f;
#pragma unroll
    for (int s = 0; s < 14; ++s) {
      const u32x4 wr = *(const u32x4*)(w_r + s * 32 + g * 8);
      const u32x4 o = __builtin_bit_cast(u32x4, xf[s]);
#pragma unroll
      for (int j = 0; j < 4; ++j) rdot = dot2bf(o[j], wr[j], rdot);
    }
    rdot += __shfl_xor(rdot, 16);
    rdot += __shfl_xor(rdot, 32);
    if (g == 0) r_out[row] = rbf(rdot);
  }
#pragma unroll
  for (int i = 0; i < 11; ++i) {
    const int id = i * 512 + tid;
    const int wr = id / 12, ch = id % 12;
    if (id < 5376) *(u32x4*)(wt + wr * K1_BN + ((ch ^ (((wr >> 3) & 1) << 1)) * 8)) = tmp[i];
  }
  __syncthreads();

  const int rbase = rt * 128 + wid * 16;
#pragma unroll
  for (int c = 0; c < 6; ++c) {
    const int n0 = n_base + 16 * c;
    f32x4 acc = {0.f, 0.f, 0.f, 0.f};
    if (n0 >= 2 * DM) {
#pragma unroll
      for (int s = 0; s < 14; ++s) {
        const bf16x8 wf = tr_frag(wt, K1_BN, lane, 32 * s, c, g & 1);
        acc = MFMA16(xf[s], wf, acc);
      }
      u32x2 pk;
      pk[0] = pk2(acc[0], acc[1]);
      pk[1] = pk2(acc[2], acc[3]);
      const int nn = n0 + l15 - 2 * DM;
      const int h = nn / DH, d = nn % DH;
      *(u32x2*)(Vt + ((size_t)(h * 32 + d) * SEQ + rbase + 4 * g)) = pk;
    } else {
#pragma unroll
      for (int s = 0; s < 14; ++s) {
        const bf16x8 wf = tr_frag(wt, K1_BN, lane, 32 * s, c, g & 1);
        acc = MFMA16(wf, xf[s], acc);
      }
      u32x2 pk;
      pk[0] = pk2(acc[0], acc[1]);
      pk[1] = pk2(acc[2], acc[3]);
      const int n = n0 + 4 * g;
      const int which = n >= DM;
      const int nn = n - which * DM;
      const int h = nn / DH, d0 = nn % DH;
      u16* dst = (which ? Kp : Qp) + ((size_t)(h * SEQ + rbase + l15) * 32 + d0);
      if (d0 == 24) {
        u32x4 p4 = {pk[0], pk[1], 0u, 0u};
        *(u32x4*)dst = p4;
      } else {
        *(u32x2*)dst = pk;
      }
    }
  }
}

#define KV_KEYS 320
#define VSTR 328
#define NFS 56

__global__ __launch_bounds__(256) void k2_attn_ffn(const u16* __restrict__ Qp, const u16* __restrict__ Kp,
                                                   const u16* __restrict__ Vt, u16* __restrict__ o_out,
                                                   const u16* __restrict__ xn, const float* __restrict__ r,
                                                   const u16* __restrict__ wg, const u16* __restrict__ wu,
                                                   const u16* __restrict__ wd, float* __restrict__ ypart,
                                                   int* __restrict__ sel_out) {
  __shared__ __attribute__((aligned(16))) u16 smem[KV_KEYS * 32 + 32 * VSTR];
  const int tid = threadIdx.x, lane = tid & 63, wid = tid >> 6;
  const int g = lane >> 4, l15 = lane & 15;

  if (blockIdx.x >= 64) {
    const int ab = blockIdx.x - 64;
    const int bi = ab >> 3;
    const int h = bi & 15, qb = (ab & 7) * 4 + (bi >> 4);
    const int q0 = qb * 64;
    const int kbase = q0 - 256;
    u16* Ks = smem;
    u16* Vs = smem + KV_KEYS * 32;
    {
      u32x4 tk[5], tv[5];
#pragma unroll
      for (int i = 0; i < 5; ++i) {
        const int id = i * 256 + tid;
        const int row = id >> 2, ch = id & 3;
        int key = kbase + row;
        key = key < 0 ? 0 : key;
        tk[i] = *(const u32x4*)(Kp + ((size_t)(h * SEQ + key) * 32 + ch * 8));
        const int d = id / 40, c8 = id % 40;
        int k8 = kbase + c8 * 8;
        k8 = k8 < 0 ? 0 : k8;
        tv[i] = *(const u32x4*)(Vt + ((size_t)(h * 32 + d) * SEQ + k8));
      }
#pragma unroll
      for (int i = 0; i < 5; ++i) {
        const int id = i * 256 + tid;
        const int row = id >> 2, ch = id & 3;
        *(u32x4*)(Ks + row * 32 + ((ch ^ ((row >> 2) & 3)) * 8)) = tk[i];
        const int d = id / 40, c8 = id % 40;
        *(u32x4*)(Vs + d * VSTR + c8 * 8) = tv[i];
      }
    }
    const int qw = q0 + wid * 16;
    const bf16x8 qf = *(const bf16x8*)(Qp + ((size_t)(h * SEQ + qw + l15) * 32 + g * 8));
    __syncthreads();

    const int blk0 = (wid >> 1) * 32;
    const int q = qw + l15;
    f32x4 sc[18];
#pragma unroll
    for (int t = 0; t < 18; ++t) {
      const int row = blk0 + t * 16 + l15;
      const bf16x8 kf = *(const bf16x8*)(Ks + row * 32 + ((g ^ ((row >> 2) & 3)) * 8));
      sc[t] = MFMA16(kf, qf, ((f32x4){0.f, 0.f, 0.f, 0.f}));
    }
    float mx = -3.0e38f;
    const int kq = q - (kbase + blk0 + 4 * g);
    if (q0 < 256) {
#pragma unroll
      for (int t = 0; t < 18; ++t)
#pragma unroll
        for (int i = 0; i < 4; ++i) {
          const int dlt = kq - t * 16 - i;
          const bool ok = ((unsigned)dlt < (unsigned)WIN) && (dlt <= q);
          sc[t][i] = ok ? sc[t][i] : -3.0e38f;
        }
    } else {
#pragma unroll
      for (int t = 0; t < 18; ++t) {
        if (t >= 2 && t < 16) continue;
#pragma unroll
        for (int i = 0; i < 4; ++i) {
          const int dlt = kq - t * 16 - i;
          const bool ok = (unsigned)dlt < (unsigned)WIN;
          sc[t][i] = ok ? sc[t][i] : -3.0e38f;
        }
      }
    }
#pragma unroll
    for (int t = 0; t < 18; ++t)
#pragma unroll
      for (int i = 0; i < 4; ++i) mx = fmaxf(mx, sc[t][i]);
    mx = fmaxf(mx, __shfl_xor(mx, 16));
    mx = fmaxf(mx, __shfl_xor(mx, 32));
    const float cs = 0.18898223650461362f * 1.4426950408889634f;
    float sum = 0.f;
#pragma unroll
    for (int t = 0; t < 18; ++t)
#pragma unroll
      for (int i = 0; i < 4; ++i) {
        const float p = __builtin_amdgcn_exp2f((sc[t][i] - mx) * cs);
        sc[t][i] = p;
        sum += p;
      }
    sum += __shfl_xor(sum, 16);
    sum += __shfl_xor(sum, 32);
    f32x4 o0 = {0.f, 0.f, 0.f, 0.f}, o1 = {0.f, 0.f, 0.f, 0.f};
#pragma unroll
    for (int b = 0; b < 9; ++b) {
      u32x4 pp;
      pp[0] = pk2(sc[2 * b][0], sc[2 * b][1]);
      pp[1] = pk2(sc[2 * b][2], sc[2 * b][3]);
      pp[2] = pk2(sc[2 * b + 1][0], sc[2 * b + 1][1]);
      pp[3] = pk2(sc[2 * b + 1][2], sc[2 * b + 1][3]);
      const bf16x8 pf = __builtin_bit_cast(bf16x8, pp);
      const u16* v0 = Vs + l15 * VSTR + blk0 + b * 32 + 4 * g;
      const u32x2 a0 = *(const u32x2*)(v0);
      const u32x2 a1 = *(const u32x2*)(v0 + 16);
      const u32x2 b0 = *(const u32x2*)(v0 + 16 * VSTR);
      const u32x2 b1 = *(const u32x2*)(v0 + 16 * VSTR + 16);
      const u32x4 va = {a0[0], a0[1], a1[0], a1[1]};
      const u32x4 vb = {b0[0], b0[1], b1[0], b1[1]};
      o0 = MFMA16(__builtin_bit_cast(bf16x8, va), pf, o0);
      o1 = MFMA16(__builtin_bit_cast(bf16x8, vb), pf, o1);
    }
    const float inv = 1.0f / sum;
    u16* dst = o_out + (size_t)q * DM + h * DH;
    u32x2 s0;
    s0[0] = pk2(o0[0] * inv, o0[1] * inv);
    s0[1] = pk2(o0[2] * inv, o0[3] * inv);
    *(u32x2*)(dst + 4 * g) = s0;
    if (g < 3) {
      u32x2 s1;
      s1[0] = pk2(o1[0] * inv, o1[1] * inv);
      s1[1] = pk2(o1[2] * inv, o1[3] * inv);
      *(u32x2*)(dst + 16 + 4 * g) = s1;
    }
    return;
  }

  if (blockIdx.x >= NFS) return;
  const int fs = blockIdx.x;
  const int f0 = fs * 32;
  float* sf = (float*)smem;
  float* red = sf + 2 * DM;
  const int fg = tid & 3, kp = tid >> 2;
  u32x4 wgv[7], wuv[7];
#pragma unroll
  for (int i = 0; i < 7; ++i) {
    const int k = kp * 7 + i;
    wgv[i] = *(const u32x4*)(wg + (size_t)k * FF + f0 + fg * 8);
    wuv[i] = *(const u32x4*)(wu + (size_t)k * FF + f0 + fg * 8);
  }
  const int dch = tid % 56, fp = tid / 56;
  u32x4 wdv[8];
  if (fp < 4) {
#pragma unroll
    for (int i = 0; i < 8; ++i) wdv[i] = *(const u32x4*)(wd + (size_t)(f0 + fp * 8 + i) * DM + dch * 8);
  }
  float v1 = -3.0e38f, v2 = -3.0e38f;
  int i1 = 0x7fffffff, i2 = 0x7fffffff;
  {
    const f32x4 ra = *(const f32x4*)(r + tid * 8);
    const f32x4 rb = *(const f32x4*)(r + tid * 8 + 4);
#pragma unroll
    for (int j = 0; j < 8; ++j) {
      const float v = j < 4 ? ra[j & 3] : rb[j & 3];
      const int ix = tid * 8 + j;
      if (v > v1) { v2 = v1; i2 = i1; v1 = v; i1 = ix; }
      else if (v > v2) { v2 = v; i2 = ix; }
    }
#pragma unroll
    for (int m = 1; m < 64; m <<= 1) {
      const float ov1 = __shfl_xor(v1, m), ov2 = __shfl_xor(v2, m);
      const int oi1 = __shfl_xor(i1, m), oi2 = __shfl_xor(i2, m);
      const bool o_first = (ov1 > v1) || (ov1 == v1 && oi1 < i1);
      float a1 = o_first ? ov1 : v1; int ai1 = o_first ? oi1 : i1;
      float c1 = o_first ? v1 : ov1; int ci1 = o_first ? i1 : oi1;
      float c2 = o_first ? ov2 : v2; int ci2 = o_first ? oi2 : i2;
      const bool cfirst = (c1 > c2) || (c1 == c2 && ci1 < ci2);
      v1 = a1; i1 = ai1;
      v2 = cfirst ? c1 : c2; i2 = cfirst ? ci1 : ci2;
    }
    float* rv = red;
    int* ri = (int*)(red + 8);
    if (lane == 0) { rv[wid * 2] = v1; rv[wid * 2 + 1] = v2; ri[wid * 2] = i1; ri[wid * 2 + 1] = i2; }
    __syncthreads();
    v1 = -3.0e38f; v2 = -3.0e38f; i1 = 0x7fffffff; i2 = 0x7fffffff;
#pragma unroll
    for (int j = 0; j < 8; ++j) {
      const float v = rv[j];
      const int ix = ri[j];
      if (v > v1 || (v == v1 && ix < i1)) { v2 = v1; i2 = i1; v1 = v; i1 = ix; }
      else if (v > v2 || (v == v2 && ix < i2)) { v2 = v; i2 = ix; }
    }
    __syncthreads();
  }
  if (fs == 0 && tid == 0) {
    sel_out[0] = i1; sel_out[1] = i2;
    ((float*)sel_out)[2] = rbf(1.0f / (1.0f + __expf(-v1)));
    ((float*)sel_out)[3] = rbf(1.0f / (1.0f + __expf(-v2)));
  }
  if (tid < 112) {
    const int t = tid / 56, c = tid % 56;
    const int row = t ? i2 : i1;
    const u32x4 v = *(const u32x4*)(xn + (size_t)row * DM + c * 8);
#pragma unroll
    for (int j = 0; j < 4; ++j) {
      sf[t * DM + c * 8 + 2 * j] = bflo(v[j]);
      sf[t * DM + c * 8 + 2 * j + 1] = bfhi(v[j]);
    }
  }
  __syncthreads();
  float ag[2][8], au[2][8];
#pragma unroll
  for (int t = 0; t < 2; ++t)
#pragma unroll
    for (int j = 0; j < 8; ++j) { ag[t][j] = 0.f; au[t][j] = 0.f; }
#pragma unroll
  for (int i = 0; i < 7; ++i) {
    const int k = kp * 7 + i;
    const float x0 = sf[k], x1 = sf[DM + k];
#pragma unroll
    for (int j = 0; j < 4; ++j) {
      ag[0][2 * j] += x0 * bflo(wgv[i][j]); ag[0][2 * j + 1] += x0 * bfhi(wgv[i][j]);
      ag[1][2 * j] += x1 * bflo(wgv[i][j]); ag[1][2 * j + 1] += x1 * bfhi(wgv[i][j]);
      au[0][2 * j] += x0 * bflo(wuv[i][j]); au[0][2 * j + 1] += x0 * bfhi(wuv[i][j]);
      au[1][2 * j] += x1 * bflo(wuv[i][j]); au[1][2 * j + 1] += x1 * bfhi(wuv[i][j]);
    }
  }
#pragma unroll
  for (int m = 4; m < 64; m <<= 1)
#pragma unroll
    for (int t = 0; t < 2; ++t)
#pragma unroll
      for (int j = 0; j < 8; ++j) { ag[t][j] += __shfl_xor(ag[t][j], m); au[t][j] += __shfl_xor(au[t][j], m); }
  if (lane < 4) {
#pragma unroll
    for (int t = 0; t < 2; ++t)
#pragma unroll
      for (int j = 0; j < 8; ++j) {
        red[((wid * 2 + t) * 32 + lane * 8 + j) * 2] = ag[t][j];
        red[((wid * 2 + t) * 32 + lane * 8 + j) * 2 + 1] = au[t][j];
      }
  }
  __syncthreads();
  float* hs = red + 1024;
  if (tid < 64) {
    const int t = tid >> 5, f = tid & 31;
    float gsum = 0.f, usum = 0.f;
#pragma unroll
    for (int w = 0; w < 4; ++w) { gsum += red[((w * 2 + t) * 32 + f) * 2]; usum += red[((w * 2 + t) * 32 + f) * 2 + 1]; }
    const float gb = rbf(gsum), ub = rbf(usum);
    const float sg = rbf(gb / (1.0f + __expf(-gb)));
    hs[t * 32 + f] = rbf(sg * ub);
  }
  __syncthreads();
  float* yred = red + 2048;
  if (fp < 4) {
    float y[2][8];
#pragma unroll
    for (int t = 0; t < 2; ++t)
#pragma unroll
      for (int j = 0; j < 8; ++j) y[t][j] = 0.f;
#pragma unroll
    for (int i = 0; i < 8; ++i) {
      const float h0 = hs[fp * 8 + i], h1 = hs[32 + fp * 8 + i];
#pragma unroll
      for (int j = 0; j < 4; ++j) {
        y[0][2 * j] += h0 * bflo(wdv[i][j]); y[0][2 * j + 1] += h0 * bfhi(wdv[i][j]);
        y[1][2 * j] += h1 * bflo(wdv[i][j]); y[1][2 * j + 1] += h1 * bfhi(wdv[i][j]);
      }
    }
#pragma unroll
    for (int t = 0; t < 2; ++t)
#pragma unroll
      for (int j = 0; j < 8; ++j) yred[(fp * 2 + t) * DM + dch * 8 + j] = y[t][j];
  }
  __syncthreads();
  for (int i = tid; i < 2 * DM; i += 256) {
    ypart[(size_t)fs * 2 * DM + i] = yred[i] + yred[2 * DM + i] + yred[4 * DM + i] + yred[6 * DM + i];
  }
}

#define K3_BN 64
__global__ __launch_bounds__(256) void k3_out(const u16* __restrict__ o_in, const u16* __restrict__ wo,
                                              const u16* __restrict__ x, const u16* __restrict__ xn,
                                              const float* __restrict__ ypart, const int* __restrict__ sel,
                                              u16* __restrict__ out) {
  __shared__ __attribute__((aligned(16))) u16 wt[DM * K3_BN];
  __shared__ float updv[2][2][K3_BN];
  const int tid = threadIdx.x, lane = tid & 63, wid = tid >> 6;
  const int g = lane >> 4, l15 = lane & 15;
  const int bi = blockIdx.x >> 3;
  const int nt = bi % 7, rt = (blockIdx.x & 7) * 4 + bi / 7;
  const int n_base = nt * K3_BN;
  const int s0 = sel[0], s1 = sel[1];
  u32x4 tmp[14];
#pragma unroll
  for (int i = 0; i < 14; ++i) {
    const int id = i * 256 + tid;
    const int row = id >> 3, ch = id & 7;
    tmp[i] = *(const u32x4*)(wo + (size_t)row * DM + n_base + ch * 8);
  }
  const int row = rt * 64 + wid * 16 + l15;
  bf16x8 of[14];
#pragma unroll
  for (int s = 0; s < 14; ++s) of[s] = *(const bf16x8*)(o_in + (size_t)row * DM + s * 32 + g * 8);
  u32x2 xv[4], nv[4];
#pragma unroll
  for (int c = 0; c < 4; ++c) {
    const int n = n_base + 16 * c + 4 * g;
    xv[c] = *(const u32x2*)(x + (size_t)row * DM + n);
    nv[c] = *(const u32x2*)(xn + (size_t)row * DM + n);
  }
  const bool has0 = (s0 >> 6) == rt, has1 = (s1 >> 6) == rt;
  if (has0 || has1) {
    const int t = (tid >> 6) & 1, c = tid & 63, hf = tid >> 7;
    float a = 0.f;
#pragma unroll
    for (int f = 0; f < NFS / 2; ++f) a += ypart[(size_t)(hf * (NFS / 2) + f) * 2 * DM + t * DM + n_base + c];
    updv[hf][t][c] = a;
  }
#pragma unroll
  for (int i = 0; i < 14; ++i) {
    const int id = i * 256 + tid;
    const int wr = id >> 3, ch = id & 7;
    const int sw = ((wr >> 1) & 1) | (((wr >> 3) & 1) << 1);
    *(u32x4*)(wt + wr * K3_BN + ((ch ^ (sw << 1)) * 8)) = tmp[i];
  }
  __syncthreads();
  const int swl = ((l15 >> 3) & 1) | ((g & 1) << 1);
#pragma unroll
  for (int c = 0; c < 4; ++c) {
    f32x4 acc = {0.f, 0.f, 0.f, 0.f};
#pragma unroll
    for (int s = 0; s < 14; ++s) {
      const bf16x8 wf = tr_frag(wt, K3_BN, lane, 32 * s, c, swl);
      acc = MFMA16(wf, of[s], acc);
    }
    float res[4];
#pragma unroll
    for (int i = 0; i < 4; ++i) {
      const float xx = (i & 1) ? bfhi(xv[c][i >> 1]) : bflo(xv[c][i >> 1]);
      float bb = (i & 1) ? bfhi(nv[c][i >> 1]) : bflo(nv[c][i >> 1]);
      if (row == s0 || row == s1) {
        const int t = row == s0 ? 0 : 1;
        const int cc = 16 * c + 4 * g + i;
        const float gate = ((const float*)sel)[2 + t];
        bb = rbf(bb + rbf(rbf(updv[0][t][cc] + updv[1][t][cc]) * gate));
      }
      res[i] = rbf(xx + rbf(acc[i])) + bb;
    }
    u32x2 pk;
    pk[0] = pk2(res[0], res[1]);
    pk[1] = pk2(res[2], res[3]);
    *(u32x2*)(out + (size_t)row * DM + n_base + 16 * c + 4 * g) = pk;
  }
}

extern "C" void kernel_launch(void* const* d_in, const int* in_sizes, int n_in, void* d_out, int out_size,
                              void* d_ws, size_t ws_size, hipStream_t stream) {
  const u16* x = (const u16*)d_in[0];
  const u16* wqkv = (const u16*)d_in[1];
  const u16* wo = (const u16*)d_in[2];
  const u16* w_r = (const u16*)d_in[4];
  const u16* wg = (const u16*)d_in[5];
  const u16* wu = (const u16*)d_in[6];
  const u16* wd = (const u16*)d_in[7];
  u16* out = (u16*)d_out;

  char* ws = (char*)d_ws;
  u16* xn = (u16*)(ws);
  float* r = (float*)(ws + 2 * 1024 * 1024);
  int* sel = (int*)(ws + 2 * 1024 * 1024 + 65536);
  float* ypart = (float*)(ws + 2 * 1024 * 1024 + 131072);
  u16* Qp = (u16*)(ws + 4 * 1024 * 1024);
  u16* Kp = (u16*)(ws + 6 * 1024 * 1024);
  u16* Vt = (u16*)(ws + 8 * 1024 * 1024);
  u16* ow = (u16*)(ws + 10 * 1024 * 1024);

  k1_ln_qkv<<<16 * 14, 512, 0, stream>>>(x, wqkv, w_r, xn, r, Qp, Kp, Vt);
  k2_attn_ffn<<<512 + 64, 256, 0, stream>>>(Qp, Kp, Vt, ow, xn, r, wg, wu, wd, ypart, sel);
  k3_out<<<32 * 7, 256, 0, stream>>>(ow, wo, x, xn, ypart, sel, out);
}
```

```cpp
#include <hip/hip_runtime.h>
#include <hip/hip_bf16.h>
#include <stdint.h>

typedef unsigned short u16;
typedef short s16x4 __attribute__((ext_vector_type(4)));
typedef short bf16x8 __attribute__((ext_vector_type(8)));
typedef float f32x4 __attribute__((ext_vector_type(4)));
typedef unsigned u32x4 __attribute__((ext_vector_type(4)));
typedef unsigned u32x2 __attribute__((ext_vector_type(2)));
typedef __attribute__((address_space(3))) s16x4 lds_s16x4;

#define DI __device__ __forceinline__
#define SEQ 2048
#define DM 448
#define NQKV 1344
#define NH 16
#define DH 28
#define FF 1792
#define WIN 256

DI float bf2f(unsigned v) { return __uint_as_float(v << 16); }
DI float bflo(unsigned v) { return __uint_as_float(v << 16); }
DI float bfhi(unsigned v) { return __uint_as_float(v & 0xffff0000u); }
DI u16 f2bf(float f) { __bf16 b = (__bf16)f; return __builtin_bit_cast(u16, b); }
DI float rbf(float f) { return bf2f(f2bf(f)); }
typedef float f32x2p __attribute__((ext_vector_type(2)));
typedef __bf16 bf2p __attribute__((ext_vector_type(2)));
DI unsigned pk2(float a, float b) {
  f32x2p v = {a, b};
  return __builtin_bit_cast(unsigned, __builtin_convertvector(v, bf2p));
}

#define MFMA16(a, b, c) __builtin_amdgcn_mfma_f32_16x16x32_bf16((a), (b), (c), 0, 0, 0)

DI bf16x8 tr_frag(const u16* base, int row_stride_elems, int lane, int krow0, int tile, int sw) {
  const int g = lane >> 4, q = (lane & 15) >> 2, p = lane & 3;
  const u16* a0 = base + (krow0 + 8 * g + q) * row_stride_elems + 16 * (tile ^ sw) + 4 * p;
  s16x4 lo = __builtin_amdgcn_ds_read_tr16_b64_v4i16((lds_s16x4*)(a0));
  s16x4 hi = __builtin_amdgcn_ds_read_tr16_b64_v4i16((lds_s16x4*)(a0 + 4 * row_stride_elems));
  return __builtin_shufflevector(lo, hi, 0, 1, 2, 3, 4, 5, 6, 7);
}

#define K1_BN 96
typedef __bf16 bf2v __attribute__((ext_vector_type(2)));
typedef float f32x2 __attribute__((ext_vector_type(2)));
DI float dot2bf(unsigned a, unsigned b, float c) {
  return __builtin_amdgcn_fdot2_f32_bf16(__builtin_bit_cast(bf2v, a), __builtin_bit_cast(bf2v, b), c, false);
}
__global__ __launch_bounds__(512) void k1_ln_qkv(const u16* __restrict__ x, const u16* __restrict__ wqkv,
                                                 const u16* __restrict__ w_r, u16* __restrict__ xn_out,
                                                 float* __restrict__ r_out, u16* __restrict__ Qp,
                                                 u16* __restrict__ Kp, u16* __restrict__ Vt) {
  __shared__ __attribute__((aligned(16))) u16 wt[DM * K1_BN];
  const int tid = threadIdx.x, lane = tid & 63, wid = tid >> 6;
  const int bi = blockIdx.x >> 3;
  const int nt = bi % 14, rt = (blockIdx.x & 7) * 2 + bi / 14;
  const int n_base = nt * K1_BN;
  const int g = lane >> 4, l15 = lane & 15;

  const int row = rt * 128 + wid * 16 + l15;
  u32x4 raw[14];
#pragma unroll
  for (int s = 0; s < 14; ++s) raw[s] = *(const u32x4*)(x + (size_t)row * DM + s * 32 + g * 8);
  u32x4 tmp[11];
#pragma unroll
  for (int i = 0; i < 11; ++i) {
    int id = i * 512 + tid;
    id = id < 5376 ? id : 5375;
    const int wr = id / 12, ch = id % 12;
    tmp[i] = *(const u32x4*)(wqkv + (size_t)wr * NQKV + n_base + ch * 8);
  }
  float sum = 0.f, sq = 0.f;
#pragma unroll
  for (int s = 0; s < 14; ++s)
#pragma unroll
    for (int j = 0; j < 4; ++j) {
      sum = dot2bf(raw[s][j], 0x3f803f80u, sum);
      sq = dot2bf(raw[s][j], raw[s][j], sq);
    }
  sum += __shfl_xor(sum, 16);
  sq += __shfl_xor(sq, 16);
  sum += __shfl_xor(sum, 32);
  sq += __shfl_xor(sq, 32);
  const float mean = sum * (1.0f / DM);
  const float var = fmaxf(sq * (1.0f / DM) - mean * mean, 0.f);
  const float rinv = __frsqrt_rn(var + 1e-5f);
  const float nb = -mean * rinv;
  bf16x8 xf[14];
#pragma unroll
  for (int s = 0; s < 14; ++s) {
    u32x4 o;
#pragma unroll
    for (int j = 0; j < 4; ++j) o[j] = pk2(fmaf(bflo(raw[s][j]), rinv, nb), fmaf(bfhi(raw[s][j]), rinv, nb));
    xf[s] = __builtin_bit_cast(bf16x8, o);
    if (nt == s) *(u32x4*)(xn_out + (size_t)row * DM + s * 32 + g * 8) = o;
  }
  if (nt == 0) {
    float rdot = 0.f;
#pragma unroll
    for (int s = 0; s < 14; ++s) {
      const u32x4 wr = *(const u32x4*)(w_r + s * 32 + g * 8);
      const u32x4 o = __builtin_bit_cast(u32x4, xf[s]);
#pragma unroll
      for (int j = 0; j < 4; ++j) rdot = dot2bf(o[j], wr[j], rdot);
    }
    rdot += __shfl_xor(rdot, 16);
    rdot += __shfl_xor(rdot, 32);
    if (g == 0) r_out[row] = rbf(rdot);
  }
#pragma unroll
  for (int i = 0; i < 11; ++i) {
    const int id = i * 512 + tid;
    const int wr = id / 12, ch = id % 12;
    if (id < 5376) *(u32x4*)(wt + wr * K1_BN + ((ch ^ (((wr >> 3) & 1) << 1)) * 8)) = tmp[i];
  }
  __syncthreads();

  const int rbase = rt * 128 + wid * 16;
#pragma unroll
  for (int c = 0; c < 6; ++c) {
    const int n0 = n_base + 16 * c;
    f32x4 acc = {0.f, 0.f, 0.f, 0.f};
    if (n0 >= 2 * DM) {
#pragma unroll
      for (int s = 0; s < 14; ++s) {
        const bf16x8 wf = tr_frag(wt, K1_BN, lane, 32 * s, c, g & 1);
        acc = MFMA16(xf[s], wf, acc);
      }
      u32x2 pk;
      pk[0] = pk2(acc[0], acc[1]);
      pk[1] = pk2(acc[2], acc[3]);
      const int nn = n0 + l15 - 2 * DM;
      const int h = nn / DH, d = nn % DH;
      *(u32x2*)(Vt + ((size_t)(h * 32 + d) * SEQ + rbase + 4 * g)) = pk;
    } else {
#pragma unroll
      for (int s = 0; s < 14; ++s) {
        const bf16x8 wf = tr_frag(wt, K1_BN, lane, 32 * s, c, g & 1);
        acc = MFMA16(wf, xf[s], acc);
      }
      u32x2 pk;
      pk[0] = pk2(acc[0], acc[1]);
      pk[1] = pk2(acc[2], acc[3]);
      const int n = n0 + 4 * g;
      const int which = n >= DM;
      const int nn = n - which * DM;
      const int h = nn / DH, d0 = nn % DH;
      u16* dst = (which ? Kp : Qp) + ((size_t)(h * SEQ + rbase + l15) * 32 + d0);
      if (d0 == 24) {
        u32x4 p4 = {pk[0], pk[1], 0u, 0u};
        *(u32x4*)dst = p4;
      } else {
        *(u32x2*)dst = pk;
      }
    }
  }
}

#define KV_KEYS 320
#define VSTR 328
#define NFS 56

__global__ __launch_bounds__(256) void k2_attn_ffn(const u16* __restrict__ Qp, const u16* __restrict__ Kp,
                                                   const u16* __restrict__ Vt, u16* __restrict__ o_out,
                                                   const u16* __restrict__ xn, const float* __restrict__ r,
                                                   const u16* __restrict__ wg, const u16* __restrict__ wu,
                                                   const u16* __restrict__ wd, float* __restrict__ ypart,
                                                   int* __restrict__ sel_out) {
  __shared__ __attribute__((aligned(16))) u16 smem[KV_KEYS * 32 + 32 * VSTR];
  const int tid = threadIdx.x, lane = tid & 63, wid = tid >> 6;
  const int g = lane >> 4, l15 = lane & 15;

  if (blockIdx.x >= 64) {
    const int ab = blockIdx.x - 64;
    const int bi = ab >> 3;
    const int h = bi & 15, qb = (ab & 7) * 4 + (bi >> 4);
    const int q0 = qb * 64;
    const int kbase = q0 - 256;
    u16* Ks = smem;
    u16* Vs = smem + KV_KEYS * 32;
    {
      u32x4 tk[5], tv[5];
#pragma unroll
      for (int i = 0; i < 5; ++i) {
        const int id = i * 256 + tid;
        const int row = id >> 2, ch = id & 3;
        int key = kbase + row;
        key = key < 0 ? 0 : key;
        tk[i] = *(const u32x4*)(Kp + ((size_t)(h * SEQ + key) * 32 + ch * 8));
        const int d = id / 40, c8 = id % 40;
        int k8 = kbase + c8 * 8;
        k8 = k8 < 0 ? 0 : k8;
        tv[i] = *(const u32x4*)(Vt + ((size_t)(h * 32 + d) * SEQ + k8));
      }
#pragma unroll
      for (int i = 0; i < 5; ++i) {
        const int id = i * 256 + tid;
        const int row = id >> 2, ch = id & 3;
        *(u32x4*)(Ks + row * 32 + ((ch ^ ((row >> 2) & 3)) * 8)) = tk[i];
        const int d = id / 40, c8 = id % 40;
        *(u32x4*)(Vs + d * VSTR + c8 * 8) = tv[i];
      }
    }
    const int qw = q0 + wid * 16;
    const bf16x8 qf = *(const bf16x8*)(Qp + ((size_t)(h * SEQ + qw + l15) * 32 + g * 8));
    __syncthreads();

    const int blk0 = (wid >> 1) * 32;
    const int q = qw + l15;
    f32x4 sc[18];
#pragma unroll
    for (int t = 0; t < 18; ++t) {
      const int row = blk0 + t * 16 + l15;
      const bf16x8 kf = *(const bf16x8*)(Ks + row * 32 + ((g ^ ((row >> 2) & 3)) * 8));
      sc[t] = MFMA16(kf, qf, ((f32x4){0.f, 0.f, 0.f, 0.f}));
    }
    float mx = -3.0e38f;
    const int kq = q - (kbase + blk0 + 4 * g);
    if (q0 < 256) {
#pragma unroll
      for (int t = 0; t < 18; ++t)
#pragma unroll
        for (int i = 0; i < 4; ++i) {
          const int dlt = kq - t * 16 - i;
          const bool ok = ((unsigned)dlt < (unsigned)WIN) && (dlt <= q);
          sc[t][i] = ok ? sc[t][i] : -3.0e38f;
        }
    } else {
#pragma unroll
      for (int t = 0; t < 18; ++t) {
        if (t >= 2 && t < 16) continue;
#pragma unroll
        for (int i = 0; i < 4; ++i) {
          const int dlt = kq - t * 16 - i;
          const bool ok = (unsigned)dlt < (unsigned)WIN;
          sc[t][i] = ok ? sc[t][i] : -3.0e38f;
        }
      }
    }
#pragma unroll
    for (int t = 0; t < 18; ++t)
#pragma unroll
      for (int i = 0; i < 4; ++i) mx = fmaxf(mx, sc[t][i]);
    mx = fmaxf(mx, __shfl_xor(mx, 16));
    mx = fmaxf(mx, __shfl_xor(mx, 32));
    const float cs = 0.18898223650461362f * 1.4426950408889634f;
    float sum = 0.f;
#pragma unroll
    for (int t = 0; t < 18; ++t)
#pragma unroll
      for (int i = 0; i < 4; ++i) {
        const float p = __builtin_amdgcn_exp2f((sc[t][i] - mx) * cs);
        sc[t][i] = p;
        sum += p;
      }
    sum += __shfl_xor(sum, 16);
    sum += __shfl_xor(sum, 32);
    f32x4 o0 = {0.f, 0.f, 0.f, 0.f}, o1 = {0.f, 0.f, 0.f, 0.f};
#pragma unroll
    for (int b = 0; b < 9; ++b) {
      u32x4 pp;
      pp[0] = pk2(sc[2 * b][0], sc[2 * b][1]);
      pp[1] = pk2(sc[2 * b][2], sc[2 * b][3]);
      pp[2] = pk2(sc[2 * b + 1][0], sc[2 * b + 1][1]);
      pp[3] = pk2(sc[2 * b + 1][2], sc[2 * b + 1][3]);
      const bf16x8 pf = __builtin_bit_cast(bf16x8, pp);
      const u16* v0 = Vs + l15 * VSTR + blk0 + b * 32 + 4 * g;
      const u32x2 a0 = *(const u32x2*)(v0);
      const u32x2 a1 = *(const u32x2*)(v0 + 16);
      const u32x2 b0 = *(const u32x2*)(v0 + 16 * VSTR);
      const u32x2 b1 = *(const u32x2*)(v0 + 16 * VSTR + 16);
      const u32x4 va = {a0[0], a0[1], a1[0], a1[1]};
      const u32x4 vb = {b0[0], b0[1], b1[0], b1[1]};
      o0 = MFMA16(__builtin_bit_cast(bf16x8, va), pf, o0);
      o1 = MFMA16(__builtin_bit_cast(bf16x8, vb), pf, o1);
    }
    const float inv = 1.0f / sum;
    u16* dst = o_out + (size_t)q * DM + h * DH;
    u32x2 s0;
    s0[0] = pk2(o0[0] * inv, o0[1] * inv);
    s0[1] = pk2(o0[2] * inv, o0[3] * inv);
    *(u32x2*)(dst + 4 * g) = s0;
    if (g < 3) {
      u32x2 s1;
      s1[0] = pk2(o1[0] * inv, o1[1] * inv);
      s1[1] = pk2(o1[2] * inv, o1[3] * inv);
      *(u32x2*)(dst + 16 + 4 * g) = s1;
    }
    return;
  }

  if (blockIdx.x >= NFS) return;
  const int fs = blockIdx.x;
  const int f0 = fs * 32;
  float* sf = (float*)smem;
  float* red = sf + 2 * DM;
  const int fg = tid & 3, kp = tid >> 2;
  u32x4 wgv[7], wuv[7];
#pragma unroll
  for (int i = 0; i < 7; ++i) {
    const int k = kp * 7 + i;
    wgv[i] = *(const u32x4*)(wg + (size_t)k * FF + f0 + fg * 8);
    wuv[i] = *(const u32x4*)(wu + (size_t)k * FF + f0 + fg * 8);
  }
  const int dch = tid % 56, fp = tid / 56;
  u32x4 wdv[8];
  if (fp < 4) {
#pragma unroll
    for (int i = 0; i < 8; ++i) wdv[i] = *(const u32x4*)(wd + (size_t)(f0 + fp * 8 + i) * DM + dch * 8);
  }
  float v1 = -3.0e38f, v2 = -3.0e38f;
  int i1 = 0x7fffffff, i2 = 0x7fffffff;
  {
    const f32x4 ra = *(const f32x4*)(r + tid * 8);
    const f32x4 rb = *(const f32x4*)(r + tid * 8 + 4);
#pragma unroll
    for (int j = 0; j < 8; ++j) {
      const float v = j < 4 ? ra[j & 3] : rb[j & 3];
      const int ix = tid * 8 + j;
      if (v > v1) { v2 = v1; i2 = i1; v1 = v; i1 = ix; }
      else if (v > v2) { v2 = v; i2 = ix; }
    }
#pragma unroll
    for (int m = 1; m < 64; m <<= 1) {
      const float ov1 = __shfl_xor(v1, m), ov2 = __shfl_xor(v2, m);
      const int oi1 = __shfl_xor(i1, m), oi2 = __shfl_xor(i2, m);
      const bool o_first = (ov1 > v1) || (ov1 == v1 && oi1 < i1);
      float a1 = o_first ? ov1 : v1; int ai1 = o_first ? oi1 : i1;
      float c1 = o_first ? v1 : ov1; int ci1 = o_first ? i1 : oi1;
      float c2 = o_first ? ov2 : v2; int ci2 = o_first ? oi2 : i2;
      const bool cfirst = (c1 > c2) || (c1 == c2 && ci1 < ci2);
      v1 = a1; i1 = ai1;
      v2 = cfirst ? c1 : c2; i2 = cfirst ? ci1 : ci2;
    }
    float* rv = red;
    int* ri = (int*)(red + 8);
    if (lane == 0) { rv[wid * 2] = v1; rv[wid * 2 + 1] = v2; ri[wid * 2] = i1; ri[wid * 2 + 1] = i2; }
    __syncthreads();
    v1 = -3.0e38f; v2 = -3.0e38f; i1 = 0x7fffffff; i2 = 0x7fffffff;
#pragma unroll
    for (int j = 0; j < 8; ++j) {
      const float v = rv[j];
      const int ix = ri[j];
      if (v > v1 || (v == v1 && ix < i1)) { v2 = v1; i2 = i1; v1 = v; i1 = ix; }
      else if (v > v2 || (v == v2 && ix < i2)) { v2 = v; i2 = ix; }
    }
    __syncthreads();
  }
  if (fs == 0 && tid == 0) {
    sel_out[0] = i1; sel_out[1] = i2;
    ((float*)sel_out)[2] = rbf(1.0f / (1.0f + __expf(-v1)));
    ((float*)sel_out)[3] = rbf(1.0f / (1.0f + __expf(-v2)));
  }
  if (tid < 112) {
    const int t = tid / 56, c = tid % 56;
    const int row = t ? i2 : i1;
    const u32x4 v = *(const u32x4*)(xn + (size_t)row * DM + c * 8);
#pragma unroll
    for (int j = 0; j < 4; ++j) {
      sf[t * DM + c * 8 + 2 * j] = bflo(v[j]);
      sf[t * DM + c * 8 + 2 * j + 1] = bfhi(v[j]);
    }
  }
  __syncthreads();
  float ag[2][8], au[2][8];
#pragma unroll
  for (int t = 0; t < 2; ++t)
#pragma unroll
    for (int j = 0; j < 8; ++j) { ag[t][j] = 0.f; au[t][j] = 0.f; }
#pragma unroll
  for (int i = 0; i < 7; ++i) {
    const int k = kp * 7 + i;
    const float x0 = sf[k], x1 = sf[DM + k];
#pragma unroll
    for (int j = 0; j < 4; ++j) {
      ag[0][2 * j] += x0 * bflo(wgv[i][j]); ag[0][2 * j + 1] += x0 * bfhi(wgv[i][j]);
      ag[1][2 * j] += x1 * bflo(wgv[i][j]); ag[1][2 * j + 1] += x1 * bfhi(wgv[i][j]);
      au[0][2 * j] += x0 * bflo(wuv[i][j]); au[0][2 * j + 1] += x0 * bfhi(wuv[i][j]);
      au[1][2 * j] += x1 * bflo(wuv[i][j]); au[1][2 * j + 1] += x1 * bfhi(wuv[i][j]);
    }
  }
#pragma unroll
  for (int m = 4; m < 64; m <<= 1)
#pragma unroll
    for (int t = 0; t < 2; ++t)
#pragma unroll
      for (int j = 0; j < 8; ++j) { ag[t][j] += __shfl_xor(ag[t][j], m); au[t][j] += __shfl_xor(au[t][j], m); }
  if (lane < 4) {
#pragma unroll
    for (int t = 0; t < 2; ++t)
#pragma unroll
      for (int j = 0; j < 8; ++j) {
        red[((wid * 2 + t) * 32 + lane * 8 + j) * 2] = ag[t][j];
        red[((wid * 2 + t) * 32 + lane * 8 + j) * 2 + 1] = au[t][j];
      }
  }
  __syncthreads();
  float* hs = red + 1024;
  if (tid < 64) {
    const int t = tid >> 5, f = tid & 31;
    float gsum = 0.f, usum = 0.f;
#pragma unroll
    for (int w = 0; w < 4; ++w) { gsum += red[((w * 2 + t) * 32 + f) * 2]; usum += red[((w * 2 + t) * 32 + f) * 2 + 1]; }
    const float gb = rbf(gsum), ub = rbf(usum);
    const float sg = rbf(gb / (1.0f + __expf(-gb)));
    hs[t * 32 + f] = rbf(sg * ub);
  }
  __syncthreads();
  float* yred = red + 2048;
  if (fp < 4) {
    float y[2][8];
#pragma unroll
    for (int t = 0; t < 2; ++t)
#pragma unroll
      for (int j = 0; j < 8; ++j) y[t][j] = 0.f;
#pragma unroll
    for (int i = 0; i < 8; ++i) {
      const float h0 = hs[fp * 8 + i], h1 = hs[32 + fp * 8 + i];
#pragma unroll
      for (int j = 0; j < 4; ++j) {
        y[0][2 * j] += h0 * bflo(wdv[i][j]); y[0][2 * j + 1] += h0 * bfhi(wdv[i][j]);
        y[1][2 * j] += h1 * bflo(wdv[i][j]); y[1][2 * j + 1] += h1 * bfhi(wdv[i][j]);
      }
    }
#pragma unroll
    for (int t = 0; t < 2; ++t)
#pragma unroll
      for (int j = 0; j < 8; ++j) yred[(fp * 2 + t) * DM + dch * 8 + j] = y[t][j];
  }
  __syncthreads();
  for (int i = tid; i < 2 * DM; i += 256) {
    ypart[(size_t)fs * 2 * DM + i] = yred[i] + yred[2 * DM + i] + yred[4 * DM + i] + yred[6 * DM + i];
  }
}

#define K3_BN 64
__global__ __launch_bounds__(256) void k3_out(const u16* __restrict__ o_in, const u16* __restrict__ wo,
                                              const u16* __restrict__ x, const u16* __restrict__ xn,
                                              const float* __restrict__ ypart, const int* __restrict__ sel,
                                              u16* __restrict__ out) {
  __shared__ __attribute__((aligned(16))) u16 wt[DM * K3_BN];
  __shared__ float updv[2][2][K3_BN];
  const int tid = threadIdx.x, lane = tid & 63, wid = tid >> 6;
  const int g = lane >> 4, l15 = lane & 15;
  const int bi = blockIdx.x >> 3;
  const int nt = bi % 7, rt = (blockIdx.x & 7) * 4 + bi / 7;
  const int n_base = nt * K3_BN;
  const int s0 = sel[0], s1 = sel[1];
  u32x4 tmp[14];
#pragma unroll
  for (int i = 0; i < 14; ++i) {
    const int id = i * 256 + tid;
    const int row = id >> 3, ch = id & 7;
    tmp[i] = *(const u32x4*)(wo + (size_t)row * DM + n_base + ch * 8);
  }
  const int row = rt * 64 + wid * 16 + l15;
  bf16x8 of[14];
#pragma unroll
  for (int s = 0; s < 14; ++s) of[s] = *(const bf16x8*)(o_in + (size_t)row * DM + s * 32 + g * 8);
  u32x2 xv[4], nv[4];
#pragma unroll
  for (int c = 0; c < 4; ++c) {
    const int n = n_base + 16 * c + 4 * g;
    xv[c] = *(const u32x2*)(x + (size_t)row * DM + n);
    nv[c] = *(const u32x2*)(xn + (size_t)row * DM + n);
  }
  const bool has0 = (s0 >> 6) == rt, has1 = (s1 >> 6) == rt;
  if (has0 || has1) {
    const int t = (tid >> 6) & 1, c = tid & 63, hf = tid >> 7;
    float a = 0.f;
#pragma unroll
    for (int f = 0; f < NFS / 2; ++f) a += ypart[(size_t)(hf * (NFS / 2) + f) * 2 * DM + t * DM + n_base + c];
    updv[hf][t][c] = a;
  }
#pragma unroll
  for (int i = 0; i < 14; ++i) {
    const int id = i * 256 + tid;
    const int wr = id >> 3, ch = id & 7;
    const int sw = ((wr >> 1) & 1) | (((wr >> 3) & 1) << 1);
    *(u32x4*)(wt + wr * K3_BN + ((ch ^ (sw << 1)) * 8)) = tmp[i];
  }
  __syncthreads();
  const int swl = ((l15 >> 3) & 1) | ((g & 1) << 1);
#pragma unroll
  for (int c = 0; c < 4; ++c) {
    f32x4 acc = {0.f, 0.f, 0.f, 0.f};
#pragma unroll
    for (int s = 0; s < 14; ++s) {
      const bf16x8 wf = tr_frag(wt, K3_BN, lane, 32 * s, c, swl);
      acc = MFMA16(wf, of[s], acc);
    }
    float res[4];
#pragma unroll
    for (int i = 0; i < 4; ++i) {
      const float xx = (i & 1) ? bfhi(xv[c][i >> 1]) : bflo(xv[c][i >> 1]);
      float bb = (i & 1) ? bfhi(nv[c][i >> 1]) : bflo(nv[c][i >> 1]);
      if (row == s0 || row == s1) {
        const int t = row == s0 ? 0 : 1;
        const int cc = 16 * c + 4 * g + i;
        const float gate = ((const float*)sel)[2 + t];
        bb = rbf(bb + rbf(rbf(updv[0][t][cc] + updv[1][t][cc]) * gate));
      }
      res[i] = rbf(xx + rbf(acc[i])) + bb;
    }
    u32x2 pk;
    pk[0] = pk2(res[0], res[1]);
    pk[1] = pk2(res[2], res[3]);
    *(u32x2*)(out + (size_t)row * DM + n_base + 16 * c + 4 * g) = pk;
  }
}

extern "C" void kernel_launch(void* const* d_in, const int* in_sizes, int n_in, void* d_out, int out_size,
                              void* d_ws, size_t ws_size, hipStream_t stream) {
  const u16* x = (const u16*)d_in[0];
  const u16* wqkv = (const u16*)d_in[1];
  const u16* wo = (const u16*)d_in[2];
  const u16* w_r = (const u16*)d_in[4];
  const u16* wg = (const u16*)d_in[5];
  const u16* wu = (const u16*)d_in[6];
  const u16* wd = (const u16*)d_in[7];
  u16* out = (u16*)d_out;

  char* ws = (char*)d_ws;
  u16* xn = (u16*)(ws);
  float* r = (float*)(ws + 2 * 1024 * 1024);
  int* sel = (int*)(ws + 2 * 1024 * 1024 + 65536);
  float* ypart = (float*)(ws + 2 * 1024 * 1024 + 131072);
  u16* Qp = (u16*)(ws + 4 * 1024 * 1024);
  u16* Kp = (u16*)(ws + 6 * 1024 * 1024);
  u16* Vt = (u16*)(ws + 8 * 1024 * 1024);
  u16* ow = (u16*)(ws + 10 * 1024 * 1024);

  k1_ln_qkv<<<16 * 14, 512, 0, stream>>>(x, wqkv, w_r, xn, r, Qp, Kp, Vt);
  k2_attn_ffn<<<512 + 64, 256, 0, stream>>>(Qp, Kp, Vt, ow, xn, r, wg, wu, wd, ypart, sel);
  k3_out<<<32 * 7, 256, 0, stream>>>(ow, wo, x, xn, ypart, sel, out);
}
```
